```python
import torch

def _softmax(x, axis=-1):
    return torch.softmax(x, dim=axis)

def _gelu(x):
    return torch.nn.functional.gelu(x, approximate='tanh')

def _silu(x):
    return torch.nn.functional.silu(x)

def _sigmoid(x):
    return torch.sigmoid(x)

def _layernorm(x, axis=-1, eps=1e-05):
    m = x.mean(dim=axis, keepdim=True)
    v = x.var(dim=axis, keepdim=True, unbiased=False)
    return (x - m) / torch.sqrt(v + eps)

def _scatter_add(tgt, idx, src, axis=0):
    return tgt.clone().index_add_(axis, idx, src)

def _segment_sum(src, seg, n):
    out = torch.zeros((n,) + src.shape[1:], dtype=src.dtype)
    return out.index_add_(0, seg, src)

def _pad(x, pw):
    flat = tuple((v for pair in reversed(pw) for v in pair))
    return torch.nn.functional.pad(x, flat)
kernel_name = 'concat_heads__memory_aug_recurrent_mem_tr__memory_aug_recurrent_mem_tr__prompt'

def setup_inputs():

    _w = lambda a: (lambda t: t.to(torch.bfloat16) if t.is_floating_point() else t)(torch.as_tensor(a))

    def _A_setup():
        B, S, Dm = (1, 2048, 640)
        H, Dh = (32, 192)
        M, seg = (8, 32)
        seg = min(seg, S)
        S = S // seg * seg
        return {'x': _w((lambda *s: torch.randn(*s, dtype=torch.bfloat16) * 0.05)(B, S, Dm)), 'mem0': _w((lambda *s: torch.randn(*s, dtype=torch.bfloat16) * 0.4)(B, M, Dm)), 'Wq': _w((lambda *s: torch.randn(*s, dtype=torch.bfloat16) * 6.4)(Dm, H, Dh) * Dm ** (-0.5)), 'Wk': _w((lambda *s: torch.randn(*s, dtype=torch.bfloat16) * 6.4)(Dm, H, Dh) * Dm ** (-0.5)), 'Wv': _w((lambda *s: torch.randn(*s, dtype=torch.bfloat16) * 0.4)(Dm, H, Dh) * Dm ** (-0.5)), 'Wo': _w((lambda *s: torch.randn(*s, dtype=torch.bfloat16) * 0.4)(H, Dh, Dm) * (H * Dh) ** (-0.5)), 'seg_len': seg}
    _A_in = _A_setup()

    def _B_setup():
        B, S, Dm = (1, 2048, 640)
        H, Dh = (32, 192)
        M, seg = (8, 32)
        seg = min(seg, S)
        S = S // seg * seg
        return {'x': _w((lambda *s: torch.randn(*s, dtype=torch.bfloat16) * 0.05)(B, S, Dm)), 'mem0': _w((lambda *s: torch.randn(*s, dtype=torch.bfloat16) * 0.4)(B, M, Dm)), 'Wq': _w((lambda *s: torch.randn(*s, dtype=torch.bfloat16) * 6.4)(Dm, H, Dh) * Dm ** (-0.5)), 'Wk': _w((lambda *s: torch.randn(*s, dtype=torch.bfloat16) * 6.4)(Dm, H, Dh) * Dm ** (-0.5)), 'Wv': _w((lambda *s: torch.randn(*s, dtype=torch.bfloat16) * 0.4)(Dm, H, Dh) * Dm ** (-0.5)), 'Wo': _w((lambda *s: torch.randn(*s, dtype=torch.bfloat16) * 0.4)(H, Dh, Dm) * (H * Dh) ** (-0.5)), 'seg_len': seg}
    _B_in = _B_setup()
    return {'A_x': _A_in['x'], 'A_mem0': _A_in['mem0'], 'A_Wq': _A_in['Wq'], 'A_Wk': _A_in['Wk'], 'A_Wv': _A_in['Wv'], 'A_Wo': _A_in['Wo'], 'A_seg_len': _A_in['seg_len'], 'B_x': _B_in['x'], 'B_mem0': _B_in['mem0'], 'B_Wq': _B_in['Wq'], 'B_Wk': _B_in['Wk'], 'B_Wv': _B_in['Wv'], 'B_Wo': _B_in['Wo'], 'B_seg_len': _B_in['seg_len']}

def reference(A_x, A_mem0, A_Wq, A_Wk, A_Wv, A_Wo, A_seg_len, B_x, B_mem0, B_Wq, B_Wk, B_Wv, B_Wo, B_seg_len):

    def _A_ref(x, mem0, Wq, Wk, Wv, Wo, seg_len):
        B, S, Dm = x.shape
        H, Dh = (Wq.shape[1], Wq.shape[2])
        M = mem0.shape[1]
        scale = Dh ** (-0.5)
        n_seg = S // seg_len
        L = M + seg_len
        idx = torch.arange(L)
        mask = (idx[:, None] < M) | (idx[:, None] >= idx[None, :])
        mem = mem0
        outs = []
        for s in range(n_seg):
            xs = x[:, s * seg_len:(s + 1) * seg_len, :].clone()
            h = torch.concatenate([mem, xs], axis=1)
            q = torch.einsum('btd,dhe->bhte', h, Wq)
            k = torch.einsum('btd,dhe->bhte', h, Wk)
            v = torch.einsum('btd,dhe->bhte', h, Wv)
            scores = torch.einsum('bhqe,bhke->bhqk', q, k) * scale
            scores = torch.where(mask[None, None], scores, -1000000000.0)
            p = _softmax(scores, axis=-1)
            o = torch.einsum('bhqk,bhke->bhqe', p, v)
            o = torch.einsum('bhte,hed->btd', o, Wo)
            mem = o[:, :M, :].clone()
            outs.append(o[:, M:, :])
        return torch.concatenate(outs, axis=1)

    def _B_ref(x, mem0, Wq, Wk, Wv, Wo, seg_len):
        B, S, Dm = x.shape
        H, Dh = (Wq.shape[1], Wq.shape[2])
        M = mem0.shape[1]
        scale = Dh ** (-0.5)
        n_seg = S // seg_len
        L = M + seg_len
        idx = torch.arange(L)
        mask = (idx[:, None] < M) | (idx[:, None] >= idx[None, :])
        mem = mem0
        outs = []
        for s in range(n_seg):
            xs = x[:, s * seg_len:(s + 1) * seg_len, :].clone()
            h = torch.concatenate([mem, xs], axis=1)
            q = torch.einsum('btd,dhe->bhte', h, Wq)
            k = torch.einsum('btd,dhe->bhte', h, Wk)
            v = torch.einsum('btd,dhe->bhte', h, Wv)
            scores = torch.einsum('bhqe,bhke->bhqk', q, k) * scale
            scores = torch.where(mask[None, None], scores, -1000000000.0)
            p = _softmax(scores, axis=-1)
            o = torch.einsum('bhqk,bhke->bhqe', p, v)
            o = torch.einsum('bhte,hed->btd', o, Wo)
            mem = o[:, :M, :].clone()
            outs.append(o[:, M:, :])
        return torch.concatenate(outs, axis=1)
    a = _A_ref(A_x, A_mem0, A_Wq, A_Wk, A_Wv, A_Wo, A_seg_len)
    b = _B_ref(B_x, B_mem0, B_Wq, B_Wk, B_Wv, B_Wo, B_seg_len)
    return torch.concatenate([a, b], axis=-1)
```

```cpp
#include <hip/hip_runtime.h>
#include <hip/hip_bf16.h>

typedef __bf16 bf2v __attribute__((ext_vector_type(2)));
typedef __bf16 bf4v __attribute__((ext_vector_type(4)));
typedef __bf16 bf8v __attribute__((ext_vector_type(8)));
typedef __bf16 bf16v __attribute__((ext_vector_type(16)));
typedef short s4v __attribute__((ext_vector_type(4)));
typedef float f2 __attribute__((ext_vector_type(2)));
typedef float f4 __attribute__((ext_vector_type(4)));
typedef unsigned u2 __attribute__((ext_vector_type(2)));
typedef unsigned u4 __attribute__((ext_vector_type(4)));
typedef unsigned u8v __attribute__((ext_vector_type(8)));
typedef __amdgpu_buffer_rsrc_t rsrc_t;
#if defined(__HIP_DEVICE_COMPILE__)
#pragma clang attribute push(__attribute__((target("no-packed-fp32-ops"))), apply_to = function)
#endif
extern __shared__ __attribute__((aligned(16))) char smem[];
typedef __attribute__((address_space(3))) char* lptr;
#define LDS0 ((lptr)smem)
#define LDSP(T, p) ((__attribute__((address_space(3))) T*)(p))
#define DI __device__ __forceinline__
#define FU(x) __builtin_bit_cast(unsigned, (float)(x))
#define UF(x) __builtin_bit_cast(float, (unsigned)(x))
template <class T> DI T tmax(T a, T b) { return a > b ? a : b; }
template <class T> DI T tmin(T a, T b) { return a < b ? a : b; }
DI lptr LP(unsigned off) { lptr p = LDS0 + off; asm("" : "+v"(p)); return p; }

constexpr int NSEG = 64;
constexpr int NL = 10;
constexpr unsigned CTRL_BYTES = 65536;
constexpr unsigned C_POIS = 0;
constexpr unsigned C_WVO = 2048;
constexpr unsigned C_PR = 7168;
constexpr unsigned C_QF = 8192;
constexpr unsigned C_TM = 32768;
constexpr unsigned MEM_SLOT = 10240, PM_SLOT = 20480, PX_SLOT = 65536, QX_SLOT = 393216;
constexpr unsigned W_MEM = 0;
constexpr unsigned W_PM = W_MEM + 66 * MEM_SLOT;
constexpr unsigned W_PX = W_PM + 65 * PM_SLOT;
constexpr unsigned W_QX = W_PX + 64 * PX_SLOT;
constexpr unsigned W_WVO = W_QX + 64 * QX_SLOT;
constexpr unsigned W_BR = W_WVO + 80 * 327680;
static_assert(W_BR % 4096 == 0, "");
constexpr int SPIN = 1 << 19;
constexpr int QK = 5;
constexpr unsigned TA0 = 249, TB0 = 166, TINC = 3;
struct Args {
  const void* x[2]; const void* mem0[2]; const void* wq[2]; const void* wk[2]; const void* wv[2]; const void* wo[2];
  void* out; void* ws;
};

DI bf8v LO(bf16v x) { return __builtin_shufflevector(x, x, 0, 1, 2, 3, 4, 5, 6, 7); }
DI bf8v HI(bf16v x) { return __builtin_shufflevector(x, x, 8, 9, 10, 11, 12, 13, 14, 15); }
DI bf16v CAT(bf8v a, bf8v b) { return __builtin_shufflevector(a, b, 0, 1, 2, 3, 4, 5, 6, 7, 8, 9, 10, 11, 12, 13, 14, 15); }
DI f4 MM(bf8v a, bf8v b, f4 c) { return __builtin_amdgcn_mfma_f32_16x16x32_bf16(a, b, c, 0, 0, 0); }
DI f4 MM16(u2 a, u2 b, f4 c) { return __builtin_amdgcn_mfma_f32_16x16x16bf16_1k(__builtin_bit_cast(s4v, a), __builtin_bit_cast(s4v, b), c, 0, 0, 0); }
DI f4 SM(bf8v a, bf16v b, f4 c, int idx) { return __builtin_amdgcn_smfmac_f32_16x16x64_bf16(a, b, c, idx, 0, 0); }
DI unsigned pk(float a, float b) { f2 v = {a, b}; return __builtin_bit_cast(unsigned, __builtin_convertvector(v, bf2v)); }
DI u2 pk4(f4 a) { u2 r = {pk(a[0], a[1]), pk(a[2], a[3])}; return r; }
DI bf8v pk8(f4 a, f4 b) { u4 r = {pk(a[0], a[1]), pk(a[2], a[3]), pk(b[0], b[1]), pk(b[2], b[3])}; return __builtin_bit_cast(bf8v, r); }
DI unsigned nosent(unsigned w) { return w == 0xFFFFFFFFu ? 0x7FFF7FFFu : w; }
DI u4 ld16(rsrc_t r, unsigned voff, unsigned soff) { return __builtin_amdgcn_raw_buffer_load_b128(r, voff, soff, 16); }
DI void st16(rsrc_t r, u4 v, unsigned voff, unsigned soff) { __builtin_amdgcn_raw_buffer_store_b128(v, r, voff, soff, 16); }
DI void st4(rsrc_t r, unsigned v, unsigned voff, unsigned soff) { __builtin_amdgcn_raw_buffer_store_b32(v, r, voff, soff, 16); }
DI unsigned ld4(rsrc_t r, unsigned voff, unsigned soff) { return __builtin_amdgcn_raw_buffer_load_b32(r, voff, soff, 16); }
DI bf8v lds8(lptr p) { return *LDSP(bf8v, p); }
DI void bar() { asm volatile("s_waitcnt lgkmcnt(0)\n\ts_barrier" ::: "memory"); }
DI void bar_vm() { asm volatile("s_waitcnt vmcnt(0) lgkmcnt(0)\n\ts_barrier" ::: "memory"); }
#define SB() __builtin_amdgcn_sched_barrier(0)
#define MIX(n, k) _Pragma("unroll") for (int i_ = 0; i_ < (n); ++i_) { __builtin_amdgcn_sched_group_barrier(8, 1, 0); __builtin_amdgcn_sched_group_barrier(0x402, k, 0); }
#define WAIT_VM(n) asm volatile("s_waitcnt vmcnt(" #n ")" ::: "memory")
DI void glds(const void* base, unsigned voff, unsigned lds) {
  unsigned sv;
  asm volatile("s_mov_b32 %0, m0\n\ts_mov_b32 m0, %3\n\ts_nop 0\n\tglobal_load_lds_dwordx4 %1, %2\n\ts_mov_b32 m0, %0"
               : "=&s"(sv) : "v"(voff), "s"(base), "s"(lds));
}
DI bf4v trd(lptr p) { return __builtin_amdgcn_ds_read_tr16_b64_v4bf16(LDSP(bf4v, p)); }
DI unsigned umax3(unsigned a, unsigned b, unsigned c) { return tmax(tmax(a, b), c); }
DI unsigned umax4(u4 a) { return tmax(umax3(a[0], a[1], a[2]), a[3]); }

#define BL "buffer_load_dwordx4 "
DI void poll3_issue(rsrc_t r, unsigned va, unsigned vb, unsigned so, u4 (&v)[3]) {
  v[0] = ld16(r, va, so); v[1] = ld16(r, va + 1024, so); v[2] = ld16(r, vb, so);
}
DI void poll3(rsrc_t r, unsigned va, unsigned vb, unsigned so, u4 (&v)[3], int& lim) {
  auto bad = [&]() { return __builtin_amdgcn_ballot_w64(umax3(umax4(v[0]), umax4(v[1]), umax4(v[2])) == 0xFFFFFFFFu) != 0; };
#pragma nounroll
  for (bool go = bad(); go; go = bad() & (--lim > 0))
    asm volatile(BL "%0, %3, %5, %6 offen sc1\n\t" BL "%1, %3, %5, %6 offen offset:1024 sc1\n\t" BL "%2, %4, %5, %6 offen sc1\n\ts_waitcnt vmcnt(0)"
                 : "+v"(v[0]), "+v"(v[1]), "+v"(v[2]) : "v"(va), "v"(vb), "s"(r), "s"(so) : "memory");
}
DI void poll5_issue(rsrc_t r, unsigned va, unsigned vb, unsigned so, u4 (&v)[5]) {
#pragma unroll
  for (int i = 0; i < 4; ++i) v[i] = ld16(r, va + i * 1280, so);
  v[4] = ld16(r, vb, so);
}
DI void poll5(rsrc_t r, unsigned va, unsigned vb, unsigned so, u4 (&v)[5], int& lim) {
  auto bad = [&]() { return __builtin_amdgcn_ballot_w64(umax3(umax3(umax4(v[0]), umax4(v[1]), umax4(v[2])), umax4(v[3]), umax4(v[4])) == 0xFFFFFFFFu) != 0; };
#pragma nounroll
  for (bool go = bad(); go; go = bad() & (--lim > 0))
    asm volatile(BL "%0, %5, %7, %8 offen sc1\n\t" BL "%1, %5, %7, %8 offen offset:1280 sc1\n\t" BL "%2, %5, %7, %8 offen offset:2560 sc1\n\t"
                 BL "%3, %5, %7, %8 offen offset:3840 sc1\n\t" BL "%4, %6, %7, %8 offen sc1\n\ts_waitcnt vmcnt(0)"
                 : "+v"(v[0]), "+v"(v[1]), "+v"(v[2]), "+v"(v[3]), "+v"(v[4]) : "v"(va), "v"(vb), "s"(r), "s"(so) : "memory");
}
DI unsigned wait_ge(rsrc_t r, unsigned off, unsigned want, int& lim) {
  unsigned v;
  while (true) {
    asm volatile("" ::: "memory");
    v = __builtin_amdgcn_readfirstlane(ld4(r, 0, off));
    if (v >= want || --lim < 0) break;
    __builtin_amdgcn_s_sleep(2);
  }
  asm volatile("" ::: "memory");
  return v;
}
DI unsigned now() { return (unsigned)__builtin_amdgcn_s_memrealtime(); }
struct Due {
  unsigned at, d;
  unsigned lo;
  DI void take(u4 v) {
    at = __builtin_amdgcn_readfirstlane(umax4(v));
    lo = __builtin_amdgcn_readfirstlane(tmin(tmin(v[0], v[1]), tmin(v[2], v[3])));
  }
  DI void set(rsrc_t ctl, unsigned off, int& lim) {
    while (!lo && --lim > 0) {
      asm volatile("" ::: "memory");
      take(ld16(ctl, 0, off));
    }
    at += d;
  }
  DI void wait() { while (at - now() - 1 < 1000u) {} }
  DI void learn(bool early) { d += early ? TINC : 0; }
};
DI f4 zero4() {
  f2 a, b;
  asm volatile("v_mov_b64 %0, 0" : "=v"(a));
  asm volatile("v_mov_b64 %0, 0" : "=v"(b));
  return __builtin_shufflevector(a, b, 0, 1, 2, 3);
}
DI void arrive(void* ws, unsigned off) {
  __hip_atomic_fetch_add((unsigned*)((char*)ws + off), 1u, __ATOMIC_RELAXED, __HIP_MEMORY_SCOPE_AGENT);
}
DI float dpp_shl4(float x) { return __builtin_bit_cast(float, __builtin_amdgcn_update_dpp(0, __builtin_bit_cast(int, x), 0x104, 0xf, 0xf, false)); }
DI unsigned odd_lane(unsigned x) { return __builtin_amdgcn_mov_dpp(x, 0xF5, 0xf, 0xf, false); }
#define DPPM " row_mask:0xf bank_mask:0xf\n\t"
#define RED4(op) "s_nop 1\n\t" op "_dpp %0, %0, %0 quad_perm:[1,0,3,2]" DPPM "s_nop 1\n\t" op "_dpp %0, %0, %0 row_ror:8" DPPM \
  "v_mov_b32 %1, %0\n\ts_nop 1\n\tv_permlane16_swap_b32 %0, %1\n\t" op " %0, %0, %1\n\t" \
  "v_mov_b32 %1, %0\n\ts_nop 1\n\tv_permlane32_swap_b32 %0, %1\n\t" op " %0, %0, %1"
DI float red4_max(float x) { float t; asm volatile(RED4("v_max_f32") : "+v"(x), "=&v"(t)); return x; }
DI float red4_sum(float x) { float t; asm volatile(RED4("v_add_f32") : "+v"(x), "=&v"(t)); return x; }
constexpr float SM_C = 0.07216878364870322f * 1.4426950408889634f;
constexpr float SM_MASK = -1e9f / 0.07216878364870322f / 4;
template <int NB> struct XR { bf8v v[NB][2]; };
template <int NB> DI void xr_load(XR<NB>& r, lptr xl, int hf) {
  if (hf < 20) { r.v[hf % NB][0] = lds8(xl + hf * 2048); r.v[hf % NB][1] = lds8(xl + hf * 2048 + 1024); }
}
template <int NB> DI void xr_start(XR<NB>& r, lptr xl) {
#pragma unroll
  for (int i = 0; i < NB - 1; ++i) xr_load(r, xl, i);
}
#define MF32 "v_mfma_f32_16x16x32_bf16 "
DI void mm_wx(f4& d, bf8v w, bf8v x, bool wa, bool first) {
  if (first) { if (wa) asm volatile(MF32 "%0, %1, %2, 0" : "=&v"(d) : "a"(w), "v"(x)); else asm volatile(MF32 "%0, %1, %2, 0" : "=&v"(d) : "v"(w), "v"(x)); }
  else { if (wa) asm volatile(MF32 "%0, %1, %2, %0" : "+v"(d) : "a"(w), "v"(x)); else asm volatile(MF32 "%0, %1, %2, %0" : "+v"(d) : "v"(w), "v"(x)); }
}
DI void mm_xw(f4& d, bf8v x, bf8v w, bool wa, bool first) {
  if (first) { if (wa) asm volatile(MF32 "%0, %1, %2, 0" : "=&v"(d) : "v"(x), "a"(w)); else asm volatile(MF32 "%0, %1, %2, 0" : "=&v"(d) : "v"(x), "v"(w)); }
  else { if (wa) asm volatile(MF32 "%0, %1, %2, %0" : "+v"(d) : "v"(x), "a"(w)); else asm volatile(MF32 "%0, %1, %2, %0" : "+v"(d) : "v"(x), "v"(w)); }
}
DI void mm32(f4& d, bf8v a, bf8v b, bool first) { mm_xw(d, a, b, false, first); }
DI void mm16(f4& d, u2 a, u2 b, bool first) {
  if (first) asm volatile("v_mfma_f32_16x16x16_bf16 %0, %1, %2, 0" : "=&v"(d) : "v"(a), "v"(b));
  else asm volatile("v_mfma_f32_16x16x16_bf16 %0, %1, %2, %0" : "+v"(d) : "v"(a), "v"(b));
}
DI void sm_w(f4& d, bf8v m, bf16v w, int idx, bool wa) {
  if (wa) asm volatile("v_smfmac_f32_16x16x64_bf16 %0, %1, %2, %3" : "+v"(d) : "v"(m), "a"(w), "v"(idx));
  else asm volatile("v_smfmac_f32_16x16x64_bf16 %0, %1, %2, %3" : "+v"(d) : "v"(m), "v"(w), "v"(idx));
}
#define GAP2(...) asm volatile("s_nop 1" : __VA_ARGS__)
#define GAP8(...) asm volatile("s_nop 7" : __VA_ARGS__)
#define P(x) "+v"(x)
DI bf8v HALF(bf16v w, int hf) { return (hf & 1) ? HI(w) : LO(w); }

DI unsigned dma_slot(int lane) { return ((lane & 3) ^ ((4 - (lane >> 4)) & 3)) * 16; }
DI unsigned frag_off(int lane) { return (lane & 15) * 64 + ((lane >> 4) ^ ((4 - ((lane >> 2) & 3)) & 3)) * 16; }
DI lptr LV(unsigned v) { return (lptr)(size_t)v; }
DI u4 x_desc(const void* x) {
  const size_t p = (size_t)x - 4096;
  u4 r = {(unsigned)p, (unsigned)(p >> 32) & 0xFFFFu, 2048u * 1280u + 4096u, 0x00020000u};
  return r;
}
DI unsigned x_voff(int w, int lane) { return (lane >> 2) * 1280 + dma_slot(lane) + w * 320; }
DI void x_base(unsigned lds, int i) { asm volatile("s_mov_b32 m0, %0" ::"s"(lds + (i >> 2) * 4096)); }
DI void x_go(u4 d, unsigned xv, int i) {
  const unsigned so = (i & 1) * 20480 + (i >> 1) * 64 + 4096 - (i & 3) * 1024;
  if ((i & 3) == 0) asm volatile("buffer_load_dwordx4 %0, %1, %2 offen lds" ::"v"(xv), "s"(d), "s"(so));
  if ((i & 3) == 1) asm volatile("buffer_load_dwordx4 %0, %1, %2 offen offset:1024 lds" ::"v"(xv), "s"(d), "s"(so));
  if ((i & 3) == 2) asm volatile("buffer_load_dwordx4 %0, %1, %2 offen offset:2048 lds" ::"v"(xv), "s"(d), "s"(so));
  if ((i & 3) == 3) asm volatile("buffer_load_dwordx4 %0, %1, %2 offen offset:3072 lds" ::"v"(xv), "s"(d), "s"(so));
}
DI void x_issue(u4 d, unsigned xv, unsigned lds) {
#pragma unroll
  for (int i = 0; i < 10; ++i) {
    if ((i & 3) == 0) { x_base(lds, i); asm volatile("s_nop 0"); }
    x_go(d, xv, i);
  }
}
struct MemSt { unsigned vb; lptr a, a2; };
DI MemSt mem_init(int w, int lane, unsigned lmem) {
  MemSt m;
  m.vb = 2048 + (lane & 31) * 16;
  m.a = LP(lmem + w * 2560 + (lane >> 4) * 256 + (lane & 7) * 16 + (lane & 8));
  m.a2 = LP(lmem + w * 2560 + 2048 + ((lane >> 4) & 1) * 256 + (lane & 7) * 16 + (lane & 8));
  return m;
}
DI void mem_put1(const MemSt& m, const u4 (&v)[3], int i) {
  lptr a = i < 2 ? m.a + i * 1024 : m.a2;
  *LDSP(u2, a) = __builtin_shufflevector(v[i], v[i], 0, 1);
  *LDSP(u2, a + 128) = __builtin_shufflevector(v[i], v[i], 2, 3);
}
DI void mem_put(const MemSt& m, const u4 (&v)[3]) { mem_put1(m, v, 0); mem_put1(m, v, 1); mem_put1(m, v, 2); }

struct WRegs { bf16v w0[3][10]; bf16v w1[3][10]; };
constexpr unsigned LA_W = 79872, LQ_W = 81920, L_XB = 0;
DI void load_weights(WRegs& R, const void* m0, const void* m1, int w, int lane, lptr wl) {
  const unsigned stg = L_XB + w * 18432;
  unsigned gv[6];
#pragma unroll
  for (int i = 0; i < 6; ++i) { int c = i * 64 + lane; gv[i] = (c / 6) * 12288 + (c % 6) * 16 + w * 96; }
  auto issue = [&](int u) {
    const char* b = (const char*)(u < 10 ? m0 : m1) + (size_t)(u % 10) * 64 * 12288;
#pragma unroll
    for (int i = 0; i < 6; ++i) glds(b, gv[i], stg + (u % 3) * 6144 + i * 1024);
  };
  issue(0); issue(1); issue(2);
  const lptr ta = LP(stg + ((lane >> 4) * 8 + ((lane >> 2) & 3)) * 96 + (lane & 3) * 8);
#pragma unroll
  for (int u = 0; u < 20; ++u) {
    if (u <= 17) WAIT_VM(12); else if (u == 18) WAIT_VM(6); else WAIT_VM(0);
    bf16v t[3];
#pragma unroll
    for (int et = 0; et < 3; ++et) {
      lptr a = ta + ((u % 3) * 6144 + et * 32);
      bf4v p0 = trd(a), p1 = trd(a + 384), p2 = trd(a + 3072), p3 = trd(a + 3072 + 384);
      t[et] = CAT(__builtin_shufflevector(p0, p1, 0, 1, 2, 3, 4, 5, 6, 7), __builtin_shufflevector(p2, p3, 0, 1, 2, 3, 4, 5, 6, 7));
    }
    asm volatile("s_waitcnt lgkmcnt(0)" ::: "memory");
    if (u + 3 < 20) issue(u + 3);
#pragma unroll
    for (int et = 0; et < 3; ++et) {
      if (u < 10) R.w0[et][u] = t[et];
      else if (et == 1) {
        lptr a = wl + (u - 10) * 2048;
        *LDSP(bf8v, a) = LO(t[et]); *LDSP(bf8v, a + 1024) = HI(t[et]);
      } else R.w1[et][u - 10] = t[et];
    }
  }
}
DI bf16v wl_get(lptr wl, int kb) { return CAT(lds8(wl + kb * 2048), lds8(wl + kb * 2048 + 1024)); }
template <int V> struct IC { static constexpr int v = V; };
template <int I, int N, class F> DI void sfor(F&& f) { if constexpr (I < N) { f(IC<I>{}); sfor<I + 1, N>(f); } }
#define LAM(x) [&](auto x) __attribute__((always_inline))
#define AT(cond) if constexpr (cond)
#define PIN(...) asm volatile("" : __VA_ARGS__)
DI void mm32c(f4& d, bf8v a, bf8v b, f4 c) { asm volatile(MF32 "%0, %1, %2, %3" : "=&v"(d) : "v"(a), "v"(b), "v"(c)); }
DI u2 L2(u4 x) { return __builtin_shufflevector(x, x, 0, 1); }
DI u2 H2(u4 x) { return __builtin_shufflevector(x, x, 2, 3); }
DI float swap_add(float q, float k) {
  auto sw = __builtin_amdgcn_permlane32_swap(FU(q), FU(k), false, false);
  return UF(sw[0]) + UF(sw[1]);
}

constexpr unsigned LA_MEM = 0, LA_SX = LA_MEM + 10240, LA_SM = LA_SX + 20480, LA_X = LA_SM + 8192;
template <class F>
DI void proj_all(const bf16v (&W)[3][10], f4 (&acc)[3][2], XR<3>& x, lptr xl, F&& fill) {
  sfor<0, 120>(LAM(n) {
    constexpr int hf = n.v / 6, i = n.v % 6;
    mm_wx(acc[i >> 1][i & 1], HALF(W[i >> 1][hf >> 1], hf), x.v[hf % 3][i & 1], true, hf == 0);
    SB();
    AT(i == 0 && hf + 2 < 20) x.v[(hf + 2) % 3][1] = lds8(xl + (hf + 2) * 2048 + 1024);
    AT(i == 1 && hf + 2 < 20) x.v[(hf + 2) % 3][0] = lds8(xl + (hf + 2) * 2048);
    AT(i >= 2) fill(IC<hf * 4 + i - 2>{});
    SB();
  });
}
struct AK { bf8v k32[2]; u4 k16; bf8v q0, q1; u4 q2; f4 x00, x01, x11; };
DI void a_tail(AK& k, f4 (&acc)[3][2], f4 bias) {
  GAP8(P(acc[0][0]), P(acc[0][1]), P(acc[1][0]), P(acc[1][1]), P(acc[2][0]), P(acc[2][1]));
  k.k32[0] = pk8(acc[0][0], acc[1][0]); k.k32[1] = pk8(acc[0][1], acc[1][1]);
  PIN(P(k.k32[0]), P(k.k32[1]));
  k.k16 = __builtin_bit_cast(u4, pk8(acc[2][0], acc[2][1]));
  PIN(P(k.k16));
  mm32c(k.x00, k.k32[0], k.q0, bias); mm32(k.x01, k.k32[0], k.q1, true); mm32c(k.x11, k.k32[1], k.q1, bias);
  mm16(k.x00, L2(k.k16), L2(k.q2), false); mm16(k.x01, L2(k.k16), H2(k.q2), false); mm16(k.x11, H2(k.k16), H2(k.q2), false);
}
DI void role_a(const Args& a, int br, int h, rsrc_t ws, rsrc_t ctl, int w, int lane) {
  int lim = SPIN;
  {
    u4 s = {~0u, ~0u, ~0u, ~0u};
    for (int i = w * 64 + lane; i < 65 * 40; i += 256) st16(ws, s, (i / 40) * PM_SLOT + (i % 40) * 16, W_PM + h * 640);
    bar_vm();
    if (w == 0 && lane == 0) arrive(a.ws, C_POIS + br * 512);
  }
  WRegs R;
  const lptr wl = LP(LA_W + w * NL * 2048 + lane * 16);
  load_weights(R, (const char*)a.wk[br] + h * 384, (const char*)a.wq[br] + h * 384, w, lane, wl);
  const int g = lane >> 4, n16 = lane & 15, blk = lane >> 3;
  unsigned xv = x_voff(w, lane);
  const u4 xd = x_desc(a.x[br]);
  const lptr xl = LP(LA_X + frag_off(lane));
  unsigned l16 = lane * 16;
  const unsigned xdst = LA_X + w * 10240;
  const lptr sxw = LP(LA_SX + w * 5120 + (n16 >> 3) * 1280 + (n16 & 7) * 16 + g * 128);
  bar();
  x_issue(xd, xv, xdst);
  f4 bias;
  unsigned long long vis[4];
#pragma unroll
  for (int r = 0; r < 4; ++r) { bias[r] = 4 * g + r <= n16 ? 0.f : SM_MASK; vis[r] = __builtin_amdgcn_ballot_w64(4 * g + r <= n16); }
  { f4 m = {SM_MASK, SM_MASK, SM_MASK, SM_MASK}; *LDSP(f4, sxw + 640) = m; }
  const unsigned qf = C_QF + ((br * 16 + (h >> 1)) * 4 + w) * 128;
  wait_ge(ctl, qf, 1, lim);
  const unsigned qo = W_QX + (h * 4 + w) * 3072;
  AK k;
  k.q0 = __builtin_bit_cast(bf8v, ld16(ws, l16, qo)); k.q1 = __builtin_bit_cast(bf8v, ld16(ws, l16 + 1024, qo)); k.q2 = ld16(ws, l16 + 2048, qo);
  bar_vm();
  {
    f4 acc[3][2];
    XR<3> xr;
    xr_start(xr, xl);
    proj_all(R.w0, acc, xr, xl, LAM(n) {});
    a_tail(k, acc, bias);
  }
  wait_ge(ctl, C_POIS + br * 512 + 256, 80, lim);
  const int idx = (lane & 8) ? 0xEEEE : 0x4444;
  const MemSt ms = mem_init(w, lane, LA_MEM);
  bf8v sel;
  {
    u4 t;
#pragma unroll
    for (int i = 0; i < 4; ++i) { const int d = n16 - 4 * (g & 1) - (i >> 1) * 8 - (i & 1) * 2; t[i] = d == 0 ? 0x3F80u : d == 1 ? 0x3F800000u : 0u; }
    sel = __builtin_bit_cast(bf8v, t);
  }
  const lptr smw = LP(LA_SM + w * 1664 + (n16 & 7) * 208 + g * 48);
  const lptr smr = LP(LA_SM + ((w & 1) * 4 + ((lane >> 1) & 3)) * 208 + (blk < 4 ? blk * 48 + (lane & 1) * 16 : 128 + (lane & 1) * 48));
  const lptr sxm = LP(LA_SX + w * 5120 + (n16 >> 3) * 1280 + (n16 & 7) * 16 + (g & 1) * 640 + 512);
  const lptr sxr = LP(LA_SX + w * 1280 + (lane < 40 ? lane : 32 + (lane & 7)) * 16);
  const int t16 = (w & 1) * 8 + (lane & 7);
  const unsigned pva = blk == 4 ? 1536 + (w >> 1) * 256 + t16 * 16 : (blk < 4 && w >= 2) ? 512 + (blk * 16 + t16) * 16 : 0x80000000u;
  const unsigned pvb = (blk < 4 && w < 2) ? (blk * 16 + t16) * 8 : 0x80000000u;
  unsigned pv = (lane < 40 && !(lane & 1)) ? lane * 8 + h * 640 + w * 320 : 0x80000000u;
  PIN(P(pv));
  Due due = {0u, TA0, 0u};
  const unsigned tmv = (h == 0 && lane == 0) ? w * 4 : 0x80000000u;
  for (int s = 0; s < NSEG; ++s) {
    u4 v[3];
    due.wait();
    SB();
    poll3_issue(ws, l16, ms.vb, W_MEM + s * MEM_SLOT + w * 2560, v);
    SB();
    bf16v wr[2];
    wr[0] = wl_get(wl, 0); wr[1] = wl_get(wl, 1);
    xv += 40960;
    PIN(P(xv));
    const int lim0 = lim;
    SB();
    mem_put1(ms, v, 0);
    SB();
    unsigned got = now();
    unsigned mxv = umax4(v[0]);
    SB();
    mem_put1(ms, v, 1);
    mxv = umax3(umax3(v[1][0], v[1][1], v[1][2]), v[1][3], mxv);
    SB();
    mem_put1(ms, v, 2);
    SB();
    if (__builtin_amdgcn_ballot_w64(umax3(umax3(v[2][0], v[2][1], v[2][2]), v[2][3], mxv) == 0xFFFFFFFFu) != 0) {
      asm volatile("s_waitcnt lgkmcnt(0)" ::: "memory");
      poll3(ws, l16, ms.vb, W_MEM + s * MEM_SLOT + w * 2560, v, lim);
      got = now();
      mem_put(ms, v);
    }
    asm volatile("s_waitcnt lgkmcnt(0)\n\ts_barrier" ::"s"(got) : "memory");
    const lptr ml = LV(l16) + LA_MEM;
    bf8v mr[3], me[5];
    unsigned qn, pmo;
    mr[0] = lds8(ml); mr[1] = lds8(ml + 1024);
    x_base(xdst, 0);
    SB();
    f4 ka[3], qa[3];
    ka[0] = zero4(); qa[0] = zero4(); ka[1] = zero4();
    SB();
#pragma unroll
    for (int kb = 0; kb < 10; ++kb) {
      const bf8v m = mr[kb % 3];
      bf8v lo, hi;
      sm_w(ka[0], m, R.w0[0][kb], idx, true);
      if (kb + 2 < 10) mr[(kb + 2) % 3] = lds8(ml + (kb + 2) * 1024);
      if (kb == 0) ka[2] = zero4();
      SB();
      sm_w(qa[0], m, R.w1[0][kb], idx, kb == 0);
      if (kb == 0) qa[2] = zero4();
      if (kb >= 1 && kb < 9) lo = lds8(wl + (kb + 1) * 2048);
      if (kb == 9) me[0] = lds8(ml + 6 * 1024);
      if (kb == 0) *LDSP(f4, sxw) = k.x00;
      SB();
      sm_w(ka[1], m, R.w0[1][kb], idx, true);
      if (kb == 0) qa[1] = zero4();
      if (kb >= 1 && kb < 9) { hi = lds8(wl + (kb + 1) * 2048 + 1024); wr[(kb + 1) & 1] = CAT(lo, hi); }
      if (kb == 9) me[1] = lds8(ml + 5 * 1024);
      if (kb == 0) *LDSP(f4, sxw + 2560) = k.x01;
      SB();
      if (kb < 2) {
        sm_w(ka[2], m, R.w0[2][kb], idx, true);
        x_go(xd, xv, 2 * kb);
        SB();
        sm_w(qa[2], m, R.w1[2][kb], idx, kb == 0);
        if (kb == 0) *LDSP(f4, sxw + 2560 + 640) = k.x11;
        SB();
      }
      sm_w(qa[1], m, wr[kb & 1], idx, false);
      if (kb < 8) x_go(xd, xv, kb < 2 ? 2 * kb + 1 : kb + 2);
      if (kb == 1 || kb == 5) x_base(xdst, kb + 3);
      if (kb == 2) st4(ctl, got | 1, tmv, C_TM + (br * 64 + s) * 128);
      if (kb == 8) qn = ld4(ctl, 0, qf);
      if (kb == 3) { pmo = W_PM + s * PM_SLOT; asm volatile("" : "+s"(pmo)); }
      if (kb == 9) due.learn(lim != lim0 && s > 1);
      SB();
    }
    f4 tr[3], d1, d2, d3, m0, m1;
    u4 vp, qku;
#pragma unroll
    for (int j = 0; j < 16; ++j) {
      const int kb = 9 - (j >> 1);
      const bf8v m = kb >= 7 ? mr[kb % 3] : me[6 - kb];
      if (j & 1) sm_w(qa[2], m, R.w1[2][kb], idx, false); else sm_w(ka[2], m, R.w0[2][kb], idx, true);
      SB();
      if (j < 3) me[2 + j] = lds8(ml + (4 - j) * 1024);
      if (j == 0) { PIN(P(qa[0]), P(ka[0])); vp[0] = pk(qa[0][0], qa[0][1]); }
      if (j == 1) vp[1] = pk(qa[0][2], qa[0][3]);
      if (j == 2) vp[2] = pk(ka[0][0], ka[0][1]);
      if (j == 3) { vp[3] = pk(ka[0][2], ka[0][3]); }
      if (j == 4) { asm volatile("s_nop 0" : P(vp)); mm32(tr[0], __builtin_bit_cast(bf8v, vp), sel, true); }
      if (j == 5) { PIN(P(qa[1]), P(ka[1])); vp[0] = pk(qa[1][0], qa[1][1]); vp[1] = pk(qa[1][2], qa[1][3]); }
      if (j == 6) { vp[2] = pk(ka[1][0], ka[1][1]); vp[3] = pk(ka[1][2], ka[1][3]); }
      if (j == 7) { asm volatile("s_nop 0" : P(vp)); mm32(tr[1], __builtin_bit_cast(bf8v, vp), sel, true); }
      if (j == 8) { PIN(P(tr[0])); qku[0] = pk(tr[0][0], tr[0][1]); qku[1] = pk(tr[0][2], tr[0][3]); }
      if (j == 11) { asm volatile("s_nop 1" : P(tr[1])); qku[2] = pk(tr[1][0], tr[1][1]); qku[3] = pk(tr[1][2], tr[1][3]); }
      if (j == 13) mm32(d2, k.k32[0], __builtin_bit_cast(bf8v, qku), true);
      SB();
    }
    const bf8v qk32 = __builtin_bit_cast(bf8v, qku);
    mm32(d3, k.k32[1], qk32, true);
    mm32(d1, qk32, qk32, true);
    asm volatile("s_nop 4" : P(ka[2]), P(qa[2]));
    vp[2] = pk(ka[2][0], ka[2][1]); vp[3] = pk(ka[2][2], ka[2][3]);
    SB();
    vp[0] = pk(qa[2][0], qa[2][1]); vp[1] = pk(qa[2][2], qa[2][3]);
    GAP2(P(vp));
    mm32(tr[2], __builtin_bit_cast(bf8v, vp), sel, true);
    mm32(m0, qk32, k.q0, true); mm32(m1, qk32, k.q1, true);
    asm volatile("s_nop 5" : P(tr[2]));
    u2 qk16 = pk4(tr[2]);
    GAP2(P(qk16));
    {
      mm16(d2, L2(k.k16), qk16, false); mm16(d3, H2(k.k16), qk16, false); mm16(d1, qk16, qk16, false);
      mm16(m0, qk16, L2(k.q2), false); mm16(m1, qk16, H2(k.q2), false);
      asm volatile("s_nop 4" : P(d1), P(d2), P(d3));
      if (n16 < 8) { *LDSP(f4, smw) = d2; *LDSP(f4, smw + 16) = d3; *LDSP(f4, smw + 32) = d1; }
      PIN(P(m0), P(m1));
      if (g >= 2) { *LDSP(f4, sxm) = m0; *LDSP(f4, sxm + 2560) = m1; }
    }
    SB();
    qn = __builtin_amdgcn_readfirstlane(qn);
    asm volatile("" : "+s"(qn));
    bar_vm();
    XR<3> xr;
    if (w < 2) {
      f4 p0 = *LDSP(f4, smr), p1 = *LDSP(f4, smr + 1664), p2 = *LDSP(f4, smr + 3328), p3 = *LDSP(f4, smr + 4992);
      SB();
      f4 t = (p0 + p1) + (p2 + p3);
      float mx = __builtin_fmaxf(__builtin_fmaxf(t[0], t[1]), __builtin_fmaxf(t[2], t[3]));
      mx = red4_max(mx) * SM_C;
#pragma unroll
      for (int j = 0; j < 4; ++j) t[j] = __builtin_amdgcn_exp2f(__builtin_fmaf(t[j], SM_C, -mx));
      float sum = lane < 40 ? (t[0] + t[1]) + (t[2] + t[3]) : 0.f;
      sum = red4_sum(sum);
      const float inv = __builtin_amdgcn_rcpf(sum);
      u4 p;
      p[0] = tmin(pk(t[0] * inv, t[1] * inv), 0xFFFFFFFEu); p[1] = tmin(pk(t[2] * inv, t[3] * inv), 0xFFFFFFFEu);
      p[2] = odd_lane(p[0]); p[3] = odd_lane(p[1]);
      st16(ws, p, pv, pmo);
    }
    xr_start(xr, xl);
    const int sn = tmin(s + 1, NSEG - 1);
    if (qn < (unsigned)sn + 1) wait_ge(ctl, qf, sn + 1, lim);
    if (w == 2 && !(h & 7) && lane == 0) st4(ctl, s + 1, 0, C_PR + (br * 4 + (h >> 3)) * 128);
    f4 acc[3][2], pa[4], t0, t1;
    float mx, sum, e0, e1;
    u4 pp, tm;
    PIN(P(l16));
    proj_all(R.w0, acc, xr, xl, LAM(n) {
      constexpr int c = n.v;
      AT(c == 0) { pa[0] = *LDSP(f4, sxr); pa[1] = *LDSP(f4, sxr + 5120); }
      AT(c == 1) { pa[2] = *LDSP(f4, sxr + 10240); pa[3] = *LDSP(f4, sxr + 15360); }
      AT(c == 2) k.q0 = __builtin_bit_cast(bf8v, ld16(ws, l16, qo + sn * QX_SLOT));
      AT(c == 4) k.q1 = __builtin_bit_cast(bf8v, ld16(ws, l16 + 1024, qo + sn * QX_SLOT));
      AT(c == 6) k.q2 = ld16(ws, l16 + 2048, qo + sn * QX_SLOT);
      AT(c >= 8 && c < 12) { pa[0][c - 8] += pa[1][c - 8]; pa[2][c - 8] += pa[3][c - 8]; }
      AT(c == 12 || c == 13) { t0[2 * c - 24] = pa[0][2 * c - 24] + pa[2][2 * c - 24]; t0[2 * c - 23] = pa[0][2 * c - 23] + pa[2][2 * c - 23]; }
      AT(c == 14) { pa[0] = *LDSP(f4, sxr + 640); pa[1] = *LDSP(f4, sxr + 5760); }
      AT(c == 15) { pa[2] = *LDSP(f4, sxr + 10880); pa[3] = *LDSP(f4, sxr + 16000); }
      AT(c == 16) { mx = __builtin_fmaxf(__builtin_fmaxf(__builtin_fmaxf(t0[0], t0[1]), t0[2]), t0[3]); }
      AT(c >= 22 && c < 26) { pa[0][c - 22] += pa[1][c - 22]; pa[2][c - 22] += pa[3][c - 22]; }
      AT(c == 26 || c == 27) { t1[2 * c - 52] = pa[0][2 * c - 52] + pa[2][2 * c - 52]; t1[2 * c - 51] = pa[0][2 * c - 51] + pa[2][2 * c - 51]; }
      AT(c == 28) { mx = __builtin_fmaxf(__builtin_fmaxf(__builtin_fmaxf(__builtin_fmaxf(mx, t1[0]), t1[1]), t1[2]), t1[3]); }
      AT(c == 30) asm volatile("v_max_f32_dpp %0, %0, %0 row_ror:8" DPPM "v_mov_b32 %1, %0" : "+v"(mx), "=&v"(e0));
      AT(c == 32) asm volatile("v_permlane16_swap_b32 %0, %1\n\tv_max_f32 %0, %0, %1" : "+v"(mx), "+v"(e0));
      AT(c == 33) asm volatile("v_mov_b32 %0, %1" : "=v"(e0) : "v"(mx));
      AT(c == 35) asm volatile("v_permlane32_swap_b32 %0, %1\n\tv_max_f32 %0, %0, %1" : "+v"(mx), "+v"(e0));
      AT(c == 36) { mx *= -SM_C; }
      AT(c == 37 || c == 38) { t0[2 * c - 74] = __builtin_fmaf(t0[2 * c - 74], SM_C, mx); t0[2 * c - 73] = __builtin_fmaf(t0[2 * c - 73], SM_C, mx); }
      AT(c == 39 || c == 40) { t1[2 * c - 78] = __builtin_fmaf(t1[2 * c - 78], SM_C, mx); t1[2 * c - 77] = __builtin_fmaf(t1[2 * c - 77], SM_C, mx); }
      AT(c >= 41 && c < 45) { t0[c - 41] = __builtin_amdgcn_exp2f(t0[c - 41]); }
      AT(c >= 45 && c < 49) { t1[c - 45] = __builtin_amdgcn_exp2f(t1[c - 45]); }
      AT(c == 49) { e0 = t0[0] + t0[1]; e1 = t0[2] + t0[3]; }
      AT(c == 50) { sum = e0 + e1; e0 = t1[0] + t1[1]; }
      AT(c == 51) { e1 = t1[2] + t1[3]; sum += e0; }
      AT(c == 52) { sum += e1; sum = lane < 40 ? sum : 0.f; }
      AT(c == 54) asm volatile("v_add_f32_dpp %0, %0, %0 row_ror:8" DPPM "v_mov_b32 %1, %0" : "+v"(sum), "=&v"(e0));
      AT(c == 56) asm volatile("v_permlane16_swap_b32 %0, %1\n\tv_add_f32 %0, %0, %1" : "+v"(sum), "+v"(e0));
      AT(c == 57) asm volatile("v_mov_b32 %0, %1" : "=v"(e0) : "v"(sum));
      AT(c == 59) asm volatile("v_permlane32_swap_b32 %0, %1\n\tv_add_f32 %0, %0, %1" : "+v"(sum), "+v"(e0));
      AT(c == 60) { sum = __builtin_amdgcn_rcpf(sum); }
      AT(c == 61 || c == 62) { t0[2 * c - 122] *= sum; t0[2 * c - 121] *= sum; }
      AT(c == 63 || c == 64) { t1[2 * c - 126] *= sum; t1[2 * c - 125] *= sum; }
      AT(c == 65) { pp[0] = pk(t0[0], t0[1]); pp[1] = pk(t0[2], t0[3]); }
      AT(c == 66) { pp[2] = pk(t1[0], t1[1]); pp[3] = pk(t1[2], t1[3]); }
      AT(c == 67) st16(ws, pp, pva, W_PX + s * PX_SLOT + h * 2048);
      AT(c == 68) { u2 ph = {pp[0], pp[1]}; __builtin_amdgcn_raw_buffer_store_b64(ph, ws, pvb, W_PX + s * PX_SLOT + h * 2048, 16); }
      AT(c == 31) tm = ld16(ctl, 0, C_TM + (br * 64 + s) * 128);
      AT(c == 58) due.take(tm);
      AT(c == 73) { e0 = SM_MASK; PIN(P(e0)); }
      AT(c == 75 || c == 77) {
        asm volatile("v_cndmask_b32_e64 %0, %2, 0, %3\n\tv_cndmask_b32_e64 %1, %2, 0, %4" : "=&v"(mx), "=&v"(sum) : "v"(e0), "s"(vis[c - 75]), "s"(vis[c - 74]));
        bias[c - 75] = mx; bias[c - 74] = sum;
      }
    });
    a_tail(k, acc, bias);
    due.set(ctl, C_TM + (br * 64 + s) * 128, lim);
  }
  bar_vm();
  if (w == 0 && lane < 40) { u4 z = {}; st16(ws, z, lane * 16, W_PM + NSEG * PM_SLOT + h * 640); }
}

DI void role_q(const Args& a, int br, int qi, rsrc_t ws, rsrc_t ctl, int w, int lane) {
  WRegs R;
  const lptr wl = LP(LQ_W + w * NL * 2048 + lane * 16);
  load_weights(R, (const char*)a.wq[br] + qi * 768, (const char*)a.wq[br] + qi * 768 + 384, w, lane, wl);
  unsigned xv = x_voff(w, lane);
  const unsigned fo = frag_off(lane);
  const unsigned qf = C_QF + ((br * 16 + qi) * 4 + w) * 128;
  bar();
  const u4 xd = x_desc(a.x[br]);
  x_issue(xd, xv, L_XB + w * 10240);
  int lim = SPIN;
  WAIT_VM(0);
  const unsigned pr = C_PR + (br * 4 + (qi >> 2)) * 128;
  int seen = 0;
  for (int s = 0; s < NSEG; ++s) {
    if (s <= 2) { WAIT_VM(0); if (lane == 0) st4(ctl, s, 0, qf); }
    else { WAIT_VM(6); if (lane == 0) st4(ctl, s - 1, 0, qf); }
    if (w == 0 && seen < s - QK) wait_ge(ctl, pr, s - QK, lim);
    bar();
    const lptr xl = LP(L_XB + (s & 1) * 40960 + fo);
    xv += 40960;
    f4 a0[3][2], a1[3][2];
    unsigned nxt;
    XR<2> xr;
    bf8v wr[2];
    xr_start(xr, xl);
    wr[0] = lds8(wl);
#pragma unroll
    for (int hf = 0; hf < 20; ++hf) {
      xr_load(xr, xl, hf + 1);
      if (hf + 1 < 20) wr[(hf + 1) & 1] = lds8(wl + (hf + 1) * 1024);
      const bf8v x0 = xr.v[hf & 1][0], x1 = xr.v[hf & 1][1];
#pragma unroll
      for (int et = 0; et < 3; ++et) {
        const bf8v u = HALF(R.w0[et][hf >> 1], hf), v = et != 1 ? HALF(R.w1[et][hf >> 1], hf) : wr[hf & 1];
        mm_wx(a0[et][0], u, x0, true, hf == 0); mm_wx(a0[et][1], u, x1, true, hf == 0);
        if (et == 0 && hf < 10 && !(hf & 3)) x_base(L_XB + ((s + 1) & 1) * 40960 + w * 10240, hf);
        if (et == 1 && hf < 10) x_go(xd, xv, hf);
        if (et == 1 && hf == 16) nxt = ld4(ctl, 0, pr);
        mm_wx(a1[et][0], v, x0, et != 1 && hf < 2, hf == 0); mm_wx(a1[et][1], v, x1, et != 1 && hf < 2, hf == 0);
      }
    }
    GAP8(P(a0[0][0]), P(a0[0][1]), P(a0[1][0]), P(a0[1][1]), P(a0[2][0]), P(a0[2][1]), P(a1[0][0]), P(a1[0][1]), P(a1[1][0]), P(a1[1][1]), P(a1[2][0]), P(a1[2][1]));
    const unsigned qo = W_QX + s * QX_SLOT + (qi * 8 + w) * 3072;
    bf8v o[6] = {pk8(a0[0][0], a0[1][0]), pk8(a0[0][1], a0[1][1]), pk8(a0[2][0], a0[2][1]), pk8(a1[0][0], a1[1][0]), pk8(a1[0][1], a1[1][1]), pk8(a1[2][0], a1[2][1])};
    PIN(P(o[0]), P(o[1]), P(o[2]), P(o[3]), P(o[4]), P(o[5]));
#pragma unroll
    for (int i = 0; i < 6; ++i) st16(ws, __builtin_bit_cast(u4, o[i]), lane * 16 + (i % 3) * 1024, qo + (i / 3) * 12288);
    seen = __builtin_amdgcn_readfirstlane(nxt);
  }
  WAIT_VM(0);
  if (lane == 0) st4(ctl, NSEG, 0, qf);
}

constexpr unsigned LB_X = 0, LB_MEM = 81920, LB_Y = LB_MEM + 10240, LB_RED = LB_Y + 40960, LB_OUT = LB_RED + 2048;
template <int H0, int H1, class F>
DI void u_part(const bf16v (&W)[4][10], f4 (&acc)[4][2], XR<3>& x, lptr xl, F&& fill) {
  sfor<H0 * 8, H1 * 8>(LAM(n) {
    constexpr int hf = n.v / 8, i = n.v % 8;
    mm_xw(acc[i >> 1][i & 1], x.v[hf % 3][i & 1], HALF(W[i >> 1][hf >> 1], hf), hf < 16, hf == 0);
    SB();
    AT(i == 0 && hf + 2 < 20) x.v[(hf + 2) % 3][1] = lds8(xl + (hf + 2) * 2048 + 1024);
    AT(i == 1 && hf + 2 < 20) x.v[(hf + 2) % 3][0] = lds8(xl + (hf + 2) * 2048);
    AT(i >= 2) fill(IC<hf * 8 + i>{});
    SB();
  });
}
#define GAPU(acc) GAP8(P(acc[0][0]), P(acc[0][1]), P(acc[1][0]), P(acc[1][1]), P(acc[2][0]), P(acc[2][1]), P(acc[3][0]), P(acc[3][1]))
struct PxCh { u2 a[2]; u4 b[2]; };
struct PxM { u4 v[4]; };
DI PxCh px_load(rsrc_t ws, unsigned so, int c, unsigned l16) {
  PxCh r;
#pragma unroll
  for (int i = 0; i < 2; ++i) {
    r.a[i] = __builtin_amdgcn_raw_buffer_load_b64(ws, l16 >> 1, so + (c * 2 + i) * 2048, 16);
    r.b[i] = ld16(ws, l16, so + (c * 2 + i) * 2048 + 512);
  }
  return r;
}
DI PxM pxm_load(rsrc_t ws, unsigned so, unsigned pvm) {
  PxM r;
#pragma unroll
  for (int i = 0; i < 4; ++i) r.v[i] = ld16(ws, pvm, so + (i >> 1) * 2048 + (i & 1) * 256);
  return r;
}
DI void px_mm(const PxCh& p, bf8v y, f4 (&o)[4], bool first) {
  const u4 yu = __builtin_bit_cast(u4, y);
  const u2 yl = {yu[0], yu[1]};
#pragma unroll
  for (int i = 0; i < 2; ++i) { mm16(o[2 * i], p.a[i], yl, first); mm32(o[2 * i + 1], __builtin_bit_cast(bf8v, p.b[i]), y, first); }
}
DI void pxm_mm(const PxM& p, bf8v y, f4 (&o)[4]) {
#pragma unroll
  for (int i = 0; i < 4; ++i) mm32(o[i], __builtin_bit_cast(bf8v, p.v[i]), y, false);
  GAP8(P(o[0]), P(o[1]), P(o[2]), P(o[3]));
}
DI void out_put(const f4 (&o)[4], lptr b, int n16) {
  *LDSP(f4, b) = n16 < 8 ? o[0] : o[2];
  *LDSP(f4, b + 64) = n16 < 8 ? o[1] : o[3];
}
DI void st2(rsrc_t r, float v, unsigned voff, unsigned soff) { __builtin_amdgcn_raw_buffer_store_b16((unsigned short)pk(v, v), r, voff, soff, 0); }
DI void role_b(const Args& a, int br, int b, rsrc_t ws, rsrc_t ctl, int w, int lane) {
  int lim = SPIN;
  const int g = lane >> 4, n16 = lane & 15;
  u4 m0v;
  asm volatile("global_load_dwordx4 %0, %1, %2" : "=v"(m0v) : "v"((lane & 7) * 1280 + b * 16), "s"(a.mem0[br]));
  {
    u4 s = {~0u, ~0u, ~0u, ~0u};
#pragma unroll
    for (int j = 0; j < 2; ++j) { const int i = 8 + j * 256 + w * 64 + lane; st16(ws, s, (i >> 3) * MEM_SLOT + (i & 7) * 16, W_MEM + b * 128); }
  }
  {
    const int W = b * 4 + w, h = W / 10, cg = W % 10;
    const unsigned stg = w * 36864;
    const char* wo = (const char*)a.wo[br] + (size_t)h * 192 * 1280 + cg * 128;
#pragma unroll
    for (int i = 0; i < 24; ++i) glds(wo + i * 8 * 1280, (lane >> 3) * 1280 + (lane & 7) * 16, stg + 12288 + i * 1024);
    const char* wv = (const char*)a.wv[br] + h * 384;
    const unsigned pst = (w >> 1) * 73728;
    unsigned vv[2];
#pragma unroll
    for (int e = 0; e < 2; ++e) { const int q = (w & 1) * 2 + e; vv[e] = (q * 8 + (lane >> 3)) * 12288 + ((lane & 7) ^ ((g & 1) * 4 + ((4 - q) & 3))) * 16; }
    auto buf = [&](int k) { return (unsigned)((k & 1) * 36864 + (k & 2) * 6144); };
    auto ldv = [&](const char* src, unsigned dst, int j) { glds(src + (j >> 1) * 128, vv[j & 1], dst + (j >> 1) * 4096 + (j & 1) * 1024); };
    const unsigned pd = pst + (w & 1) * 2048;
#pragma unroll
    for (int j = 0; j < 12; ++j) ldv(wv + (j / 6) * 32 * 12288, pd + buf(j / 6), j % 6);
    asm volatile("s_waitcnt vmcnt(12)" : P(m0v) :: "memory");
    { u4 v = {nosent(m0v[0]), nosent(m0v[2]), nosent(m0v[1]), nosent(m0v[3])}; st16(ws, v, (w == 0 && lane < 8) ? lane * 16 : 0x80000000u, W_MEM + b * 128); }
    bf8v bo[6][4];
    const lptr ta = LP(stg + 12288 + (g * 8 + ((lane >> 2) & 3)) * 128 + (lane & 3) * 8);
#pragma unroll
    for (int ks = 0; ks < 6; ++ks)
#pragma unroll
      for (int ct = 0; ct < 4; ++ct) {
        bf4v p0 = trd(ta + ks * 4096 + ct * 32), p1 = trd(ta + ks * 4096 + ct * 32 + 512);
        bo[ks][ct] = __builtin_shufflevector(p0, p1, 0, 1, 2, 3, 4, 5, 6, 7);
      }
    bar();
#pragma unroll
    for (int j = 12; j < 24; ++j) ldv(wv + (j / 6) * 32 * 12288, pd + buf(j / 6), j % 6);
    const unsigned ov = (n16 >> 3) * 327680 + g * 128 + (n16 & 7) * 16;
    const unsigned oo = W_WVO + cg * 8 * 327680 + h * 10240;
    const unsigned vo = pst + (n16 >> 2) * 1024 + (n16 & 3) * 128 + (g ^ ((n16 & 2) * 2 + ((4 - (n16 >> 2)) & 3))) * 16;
    auto rd = [&](lptr va, lptr vb, int j) { return lds8(((j & 2) ? vb : va) + ((j >> 2) * 4096 + (j & 1) * 512)); };
    const u4 vd = {(unsigned)(size_t)wv, (unsigned)((size_t)wv >> 32) & 0xFFFFu, 640u * 12288u - h * 384u, 0x00020000u};
    const unsigned vv1 = vv[1] - 1024;
    unsigned ovk = 0x80000000u;
    lptr va = LP(vo + buf(1)), vb = LP((vo ^ 64) + buf(1));
    auto round = [&](int k, const bf8v (&av)[12], bf8v (&nx)[12], f4 (&acc)[4][2], f4 (&old)[4][2]) {
      asm volatile("s_waitcnt vmcnt(12) lgkmcnt(0)\n\ts_barrier" ::: "memory");
      u4 o[4];
      unsigned src, dst, so;
      SB();
#pragma unroll
      for (int i = 0; i < 48; ++i) {
        const int ks = i >> 3, ct = (i >> 1) & 3, c = i / 3 * 2 + i % 3 - 3, q = (i - 9) / 6;
        mm_xw(acc[ct][i & 1], av[2 * ks + (i & 1)], bo[ks][ct], true, ks == 0);
        SB();
        if (i < 36 && i % 3 == 0) nx[i / 3] = rd(va, vb, i / 3);
        if (i == 1) PIN(P(old[0][0]), P(old[0][1]), P(old[1][0]), P(old[1][1]), P(old[2][0]), P(old[2][1]), P(old[3][0]), P(old[3][1]));
        if (i == 2) { so = oo + (k - 1) * 512; asm volatile("" : "+s"(so)); }
        if (i >= 4 && i < 27 && i % 3) { const f4 r = old[c >> 2][(c >> 1) & 1]; o[c >> 2][c & 3] = pk(r[(c & 1) * 2], r[(c & 1) * 2 + 1]); }
        if (i >= 9 && i < 28 && i % 6 == 3) st16(ws, o[q], ovk, so + q * 2 * 327680);
        if (i == 28) { src = tmin(k + 4, 19) * (32 * 12288); asm volatile("" : "+s"(src)); }
        if (i == 30) PIN(P(o[0]), P(o[1]), P(o[2]), P(o[3]));
        if (i == 29) { dst = pd + buf(k); asm volatile("" : "+s"(dst)); }
        if (i >= 31 && i < 40) {
          const int p = (i - 31) / 3;
          if (i % 3 == 1) asm volatile("s_mov_b32 m0, %0" ::"s"(dst + p * 4096));
          if (i % 3 == 2) asm volatile("buffer_load_dwordx4 %0, %1, %2 offen lds" ::"v"(vv[0]), "s"(vd), "s"(src + p * 128));
          if (i % 3 == 0) asm volatile("buffer_load_dwordx4 %0, %1, %2 offen offset:1024 lds" ::"v"(vv1), "s"(vd), "s"(src + p * 128));
        }
        if (i == 41) { dst = buf(k + 2); asm volatile("" : "+s"(dst)); }
        if (i == 42) { va = LV(vo + dst); PIN(P(va)); }
        if (i == 43) { vb = LV((vo ^ 64) + dst); PIN(P(vb)); }
        if (i == 44) { ovk = ov; PIN(P(ovk)); }
        SB();
      }
    };
    bf8v a0[12], a1[12];
    f4 c0[4][2], c1[4][2];
#pragma unroll
    for (int ct = 0; ct < 4; ++ct) { c1[ct][0] = zero4(); c1[ct][1] = zero4(); }
    WAIT_VM(12);
    bar();
    if (w == 0 && lane == 0) arrive(a.ws, C_POIS + br * 512 + 256);
    {
      const lptr ua = LP(vo), ub = LP(vo ^ 64);
#pragma unroll
      for (int j = 0; j < 12; ++j) a0[j] = rd(ua, ub, j);
    }
#pragma nounroll
    for (int k = 0; k < 20; k += 2) { round(k, a0, a1, c0, c1); round(k + 1, a1, a0, c1, c0); }
    GAP8(P(c1[0][0]), P(c1[0][1]), P(c1[1][0]), P(c1[1][1]), P(c1[2][0]), P(c1[2][1]), P(c1[3][0]), P(c1[3][1]));
#pragma unroll
    for (int ct = 0; ct < 4; ++ct) { st16(ws, __builtin_bit_cast(u4, pk8(c1[ct][0], c1[ct][1])), ov, oo + 19 * 512 + ct * 2 * 327680); SB(); asm volatile("s_nop 1"); SB(); }
    WAIT_VM(0);
    if (lane == 0) arrive(a.ws, C_WVO + (br * 10 + cg) * 256);
  }
  unsigned xv = x_voff(w, lane);
  const unsigned fo = frag_off(lane);
  const u4 xd = x_desc(a.x[br]);
  bar();
  x_issue(xd, xv, LB_X + w * 10240);
  xv += 40960;
  x_issue(xd, xv, LB_X + 40960 + w * 10240);
  wait_ge(ctl, C_POIS + br * 512, 32, lim);
  wait_ge(ctl, C_POIS + br * 512 + 256, 80, lim);
  wait_ge(ctl, C_WVO + (br * 10 + (b >> 3)) * 256, 32, lim);
  bar_vm();
  bf16v W[4][10];
  {
    const unsigned wvv = (n16 >> 3) * 10240 + g * 128 + (n16 & 7) * 16, wo = W_WVO + b * 327680 + w * 81920;
#pragma unroll
    for (int kb = 0; kb < 10; ++kb)
#pragma unroll
      for (int t = 0; t < 4; ++t)
        W[t][kb] = CAT(__builtin_bit_cast(bf8v, ld16(ws, wvv, wo + kb * 1024 + t * 20480)), __builtin_bit_cast(bf8v, ld16(ws, wvv, wo + kb * 1024 + 512 + t * 20480)));
  }
  SB();
  bf8v y[4];
  {
    f4 acc[4][2];
    XR<3> xr;
    const lptr xl = LP(LB_X + fo);
    xr_start(xr, xl);
    u_part<0, 20>(W, acc, xr, xl, LAM(n) {});
    GAPU(acc);
#pragma unroll
    for (int t = 0; t < 4; ++t) y[t] = pk8(acc[t][0], acc[t][1]);
  }
  bar();
  const int idx = (lane & 8) ? 0xEEEE : 0x4444;
  const int pg = ((g & 1) << 1) | (g >> 1);
  unsigned l16 = lane * 16, l8 = lane * 8, pvm = pg * 4096 + 1536 + n16 * 16;
  unsigned pma = (n16 >> 2) * 320 + g * 64 + (n16 & 3) * 16;
  const unsigned pmb = (pg * 4 + (n16 >> 2)) * 320 + 256 + (n16 & 3) * 16;
  const rsrc_t ro = __builtin_amdgcn_make_buffer_rsrc(a.out, 0, 2048 * 1280 * 2, 0x00020000);
  const unsigned outv = ((w * 8 + (lane >> 3)) * 1280 + br * 640 + b * 8 + (lane & 7)) * 2;
  const lptr outl = LP(LB_OUT + (lane & 7) * 144 + (w * 8 + (lane >> 3)) * 4), meml = LP(LB_MEM + lane * 16);
  const lptr outw = LP(LB_OUT + (w * 16 + n16) * 144 + g * 16);
  const MemSt ms = mem_init(w, lane, LB_MEM);
  const lptr redl = LP(LB_RED + lane * 4), redw = LP(LB_RED + w * 512 + g * 128 + (n16 & 7) * 16);
  const unsigned pubv = (lane & 28) ? 0x80000000u : ((lane >> 5) * 4 + (lane & 3)) * 16;
  Due due = {0u, TB0, 0u};
  for (int s = 0; s < NSEG; ++s) {
    xv += 40960;
    const lptr xl = LP(LB_X + ((s + 1) & 1) * 40960 + fo);
    const lptr yl = LP(LB_Y + ((s & 1) * 4 + w) * 5120 + lane * 16);
    f4 acc[4][2], o[4];
    const unsigned pxo = W_PX + (s - 2) * PX_SLOT + w * 16384, mo = W_MEM + s * MEM_SLOT + w * 2560;
    XR<3> xr;
    xr_start(xr, xl);
    PIN(P(l16), P(l8), P(pvm));
    const unsigned xb = LB_X + (s & 1) * 40960 + w * 10240;
    x_base(xb, 0);
    u2 pa[8];
    u4 pb[8], pm[4], v[3], tm;
    bf8v yo[5], mr[3];
    float op[8];
    f4 z[4];
    auto px = LAM(n) {
      constexpr int h = n.v >> 1;
      AT(n.v < 16 && !(n.v & 1)) pa[h] = __builtin_amdgcn_raw_buffer_load_b64(ws, l8 + (h & 1) * 2048, pxo + (h >> 1) * 4096, 16);
      AT(n.v < 16 && (n.v & 1)) pb[h] = ld16(ws, l16 + (h & 1) * 2048 + 512, pxo + (h >> 1) * 4096);
      AT(n.v >= 16) pm[n.v - 16] = ld16(ws, pvm + (h - 8) * 2048 + (n.v & 1) * 256, pxo);
    };
    auto pxm = LAM(n) {
      constexpr int t = n.v >> 2, q = n.v & 3;
      AT(t < 4 && !(q & 1)) mm16(o[q], pa[2 * t + (q >> 1)], L2(__builtin_bit_cast(u4, yo[t])), t == 0);
      AT(t < 4 && (q & 1)) mm32(o[q], __builtin_bit_cast(bf8v, pb[2 * t + (q >> 1)]), yo[t], t == 0);
      AT(t == 4) mm32(o[q], __builtin_bit_cast(bf8v, pm[q]), yo[4], false);
    };
    u_part<0, 14>(W, acc, xr, xl, LAM(n) {
      constexpr int hf = n.v >> 3, i = n.v & 7, e = hf * 2 + (i == 5);
      AT(hf == 0 && (i == 3 || i == 4 || i >= 6)) {
        constexpr int k = i < 5 ? 2 * i - 6 : 2 * i - 8;
        op[k] = *LDSP(float, outl + k * 1152); op[k + 1] = *LDSP(float, outl + (k + 1) * 1152);
      }
      AT(hf == 1 && (i == 4 || i == 6)) { op[2 * i - 8] += op[2 * i - 7]; op[2 * i - 6] += op[2 * i - 5]; }
      AT(hf == 1 && i == 7) { op[0] += op[2]; op[4] += op[6]; }
      AT(hf == 2 && i == 3) op[0] += op[4];
      AT(hf == 2 && i == 4) st2(ro, op[0], outv, (s - 3) * 81920);
      AT(i == 2 || i == 5) {
        AT(e < 10) x_go(xd, xv, e);
        AT(e == 3 || e == 7) x_base(xb, e + 1);
        AT(e >= 10 && e < 20) px(IC<e - 10>{});
        AT(e == 20) v[0] = ld16(ws, l16, mo);
        AT(e == 21) v[1] = ld16(ws, l16 + 1024, mo);
        AT(e == 22) v[2] = ld16(ws, ms.vb, mo);
        AT(e >= 23) px(IC<e - 13>{});
      }
      AT((hf == 10 || hf == 12) && i == 3) yo[(hf - 10) >> 1] = lds8(yl + ((hf - 10) >> 1) * 1024);
      AT((hf == 11 || hf == 13) && (i == 3 || i == 4 || i >= 6)) pxm(IC<(hf - 11) * 2 + (i < 5 ? i - 3 : i - 4)>{});
      AT(hf == 12 && i == 7) *LDSP(bf8v, yl) = y[0];
    });
    poll3(ws, l16, ms.vb, mo, v, lim);
    u_part<14, 15>(W, acc, xr, xl, LAM(n) {
      constexpr int i = n.v & 7;
      AT(i >= 2 && i < 5) mem_put1(ms, v, i - 2);
      AT(i == 5) *LDSP(bf8v, yl + 1024) = y[1];
      AT(i == 6) px(IC<15>{});
    });
    bar();
    u_part<15, 16>(W, acc, xr, xl, LAM(n) {
      constexpr int i = n.v & 7;
      AT(i == 2) { mr[0] = lds8(meml); mr[1] = lds8(meml + 1024); }
      AT(i >= 3 && i < 7) z[i - 3] = zero4();
      AT(i == 7) px(IC<16>{});
    });
    sfor<0, 40>(LAM(n) {
      constexpr int kb = n.v >> 2, t = n.v & 3;
      sm_w(z[t], mr[kb % 3], W[t][kb], idx, kb < 8);
      SB();
      AT(t == 0 && kb + 2 < 10) mr[(kb + 2) % 3] = lds8(meml + (kb + 2) * 1024);
      AT(t == 2 && kb < 3) px(IC<17 + kb>{});
      AT(t == 2 && kb == 4) tm = ld16(ctl, 0, C_TM + (br * 64 + s) * 128);
      AT(t == 1 && (kb == 2 || kb == 5 || kb == 8)) yo[(kb + 4) / 3] = lds8(yl + (kb + 4) / 3 * 1024);
      AT(kb == 3 || kb == 6) pxm(IC<(kb + 3) / 3 * 4 + t>{});
      AT(t == 1 && (kb == 4 || kb == 7)) *LDSP(bf8v, yl + (kb + 2) / 3 * 1024) = y[(kb + 2) / 3];
      SB();
    });
    PIN(P(pma));
    const unsigned po = W_PM + s * PM_SLOT + w * 5120;
    f4 r1, r2;
    u2 a1, a2;
    u4 p[5], zz;
    due.take(tm);
    due.set(ctl, C_TM + (br * 64 + s) * 128, lim);
    due.wait();
    SB();
    poll5_issue(ws, pma, pmb, po, p);
    SB();
    u_part<16, 20>(W, acc, xr, xl, LAM(n) {
      constexpr int c = n.v - 16 * 8;
      AT(c >= 2 && c < 6) pxm(IC<14 + c>{});
      AT(c == 6) PIN(P(z[0]), P(z[1]), P(z[2]), P(z[3]));
      AT(c == 6 || c == 7) r1[c - 6] = swap_add(z[0][c - 6], z[1][c - 6]);
      AT(c == 10 || c == 11) r1[c - 8] = swap_add(z[0][c - 8], z[1][c - 8]);
      AT(c >= 12 && c < 16) r2[c - 12] = swap_add(z[2][c - 12], z[3][c - 12]);
      AT(c == 18) a1 = pk4(r1);
      AT(c == 19) a2 = pk4(r2);
      AT(c == 21) {
        auto e0 = __builtin_amdgcn_permlane16_swap(a1[0], a2[0], false, false);
        auto e1 = __builtin_amdgcn_permlane16_swap(a1[1], a2[1], false, false);
        zz[0] = e0[0]; zz[1] = e1[0]; zz[2] = e0[1]; zz[3] = e1[1];
      }
      AT(c == 20) PIN(P(o[0]), P(o[1]), P(o[2]), P(o[3]));
      AT(c == 20 || c == 22 || c == 23 || c == 26) {
        constexpr int k = c == 20 ? 0 : c == 26 ? 3 : c - 21, q = k >> 1, r = (k & 1) * 2;
        o[q][r] = n16 < 8 ? o[q][r] : o[q + 2][r]; o[q][r + 1] = n16 < 8 ? o[q][r + 1] : o[q + 2][r + 1];
      }
      AT(c == 27) *LDSP(u4, yl + 4096) = zz;
      AT(c == 28) *LDSP(f4, outw) = o[0];
      AT(c == 29) *LDSP(f4, outw + 64) = o[1];
    });
    SB();
    {
      f4 d0;
      unsigned mx = 0;
#pragma unroll
      for (int t = 0; t < 5; ++t) {
        mm32(d0, __builtin_bit_cast(bf8v, p[t]), t < 4 ? y[t] : __builtin_bit_cast(bf8v, zz), t == 0);
        SB();
        mx = umax3(umax3(p[t][0], p[t][1], p[t][2]), p[t][3], mx);
        SB();
      }
      if (__builtin_amdgcn_ballot_w64(mx == 0xFFFFFFFFu) != 0) {
        GAP8(P(d0));
        SB();
        poll5(ws, pma, pmb, po, p, lim);
#pragma unroll
        for (int t = 0; t < 5; ++t) mm32(d0, __builtin_bit_cast(bf8v, p[t]), t < 4 ? y[t] : __builtin_bit_cast(bf8v, zz), t == 0);
        due.learn(s > 0);
        asm volatile("s_nop 3" : P(d0));
      }
      asm volatile("s_nop 4" : P(d0));
      if ((n16 >> 3) == (g >> 1)) *LDSP(f4, redw) = d0;
    }
    bar();
    if (w == 0) {
      float q[8];
#pragma unroll
      for (int i = 0; i < 8; ++i) q[i] = *LDSP(float, redl + i * 256);
      SB();
      float v = ((q[0] + q[1]) + (q[2] + q[3])) + ((q[4] + q[5]) + (q[6] + q[7]));
      const unsigned d = tmin(pk(v, dpp_shl4(v)), 0xFFFFFFFEu), e = __builtin_amdgcn_mov_dpp(d, 0x108, 0xf, 0xf, false);
      auto s0 = __builtin_amdgcn_permlane16_swap(d, FU(q[0]), false, false);
      auto s1 = __builtin_amdgcn_permlane16_swap(e, FU(q[1]), false, false);
      u4 pb4 = {s0[0], s0[1], s1[0], s1[1]};
      st16(ws, pb4, pubv, W_MEM + (s + 1) * MEM_SLOT + b * 128);
    }
    SB();
    bf8v yn[4];
    PIN(P(acc[0][0]), P(acc[0][1]), P(acc[1][0]), P(acc[1][1]), P(acc[2][0]), P(acc[2][1]), P(acc[3][0]), P(acc[3][1]));
#pragma unroll
    for (int t = 0; t < 4; ++t) yn[t] = pk8(acc[t][0], acc[t][1]);
#pragma unroll
    for (int t = 0; t < 4; ++t) y[t] = yn[t];
  }
  auto out_sum = [&](int seg) {
    float q[8];
#pragma unroll
    for (int i = 0; i < 8; ++i) q[i] = *LDSP(float, outl + i * 1152);
    st2(ro, ((q[0] + q[1]) + (q[2] + q[3])) + ((q[4] + q[5]) + (q[6] + q[7])), outv, seg * 81920);
  };
  out_sum(NSEG - 3);
  PIN(P(pma));
  for (int s = NSEG; s < NSEG + 2; ++s) {
    if (s == NSEG + 1) {
      u4 p[5];
      poll5_issue(ws, pma, pmb, W_PM + NSEG * PM_SLOT + w * 5120, p);
      poll5(ws, pma, pmb, W_PM + NSEG * PM_SLOT + w * 5120, p, lim);
    }
    const lptr yl = LP(LB_Y + ((s & 1) * 4 + w) * 5120 + lane * 16);
    f4 o[4];
    const unsigned pxo = W_PX + (s - 2) * PX_SLOT + w * 16384;
    PxCh c[4];
#pragma unroll
    for (int i = 0; i < 4; ++i) c[i] = px_load(ws, pxo, i, l16);
    PxM cm = pxm_load(ws, pxo, pvm);
    SB();
#pragma unroll
    for (int i = 0; i < 4; ++i) px_mm(c[i], lds8(yl + i * 1024), o, i == 0);
    pxm_mm(cm, lds8(yl + 4096), o);
    bar();
    out_put(o, outw, n16);
    bar();
    out_sum(s - 2);
  }
}

__global__ void __launch_bounds__(256, 1) rmt_kernel(Args a) {
  const int bid = blockIdx.x, xcd = bid & 7, idx = bid >> 3, br = xcd >> 2, xq = xcd & 3;
  const int w = __builtin_amdgcn_readfirstlane(threadIdx.x >> 6), lane = threadIdx.x & 63;
  rsrc_t ctl = __builtin_amdgcn_make_buffer_rsrc(a.ws, 0, CTRL_BYTES, 0x00020000);
  rsrc_t ws = __builtin_amdgcn_make_buffer_rsrc((char*)a.ws + CTRL_BYTES + (size_t)br * W_BR, 0, W_BR, 0x00020000);
  if (idx < 8) role_a(a, br, xq * 8 + idx, ws, ctl, w, lane);
  else if (idx < 28) role_b(a, br, xq * 20 + idx - 8, ws, ctl, w, lane);
  else role_q(a, br, xq * 4 + idx - 28, ws, ctl, w, lane);
}

#if defined(__HIP_DEVICE_COMPILE__)
#pragma clang attribute pop
#endif

extern "C" void kernel_launch(void* const* d_in, const int* in_sizes, int n_in, void* d_out, int out_size, void* d_ws,
                              size_t ws_size, hipStream_t stream) {
  Args a;
  for (int b = 0; b < 2; ++b) {
    a.x[b] = d_in[b * 7 + 0]; a.mem0[b] = d_in[b * 7 + 1]; a.wq[b] = d_in[b * 7 + 2];
    a.wk[b] = d_in[b * 7 + 3]; a.wv[b] = d_in[b * 7 + 4]; a.wo[b] = d_in[b * 7 + 5];
  }
  a.out = d_out; a.ws = d_ws;
  (void)hipFuncSetAttribute((const void*)rmt_kernel, hipFuncAttributeMaxDynamicSharedMemorySize, 163840);
  (void)hipMemsetAsync(d_ws, 0, CTRL_BYTES, stream);
  hipLaunchKernelGGL(rmt_kernel, dim3(256), dim3(256), 163840, stream, a);
}
```
